```python
import torch

def _softmax(x, axis=-1):
    return torch.softmax(x, dim=axis)
def _gelu(x):
    return torch.nn.functional.gelu(x, approximate="tanh")
def _silu(x):
    return torch.nn.functional.silu(x)
def _sigmoid(x):
    return torch.sigmoid(x)
def _layernorm(x, axis=-1, eps=1e-5):
    m = x.mean(dim=axis, keepdim=True)
    v = x.var(dim=axis, keepdim=True, unbiased=False)
    return (x - m) / torch.sqrt(v + eps)
def _scatter_add(tgt, idx, src, axis=0):
    return tgt.clone().index_add_(axis, idx, src)
def _segment_sum(src, seg, n):
    out = torch.zeros((n,) + src.shape[1:], dtype=src.dtype)
    return out.index_add_(0, seg, src)
def _pad(x, pw):
    flat = tuple(v for pair in reversed(pw) for v in pair)
    return torch.nn.functional.pad(x, flat)

kernel_name = 'interleave_layers__linear_kernel_hedgehog__spectral_fnet__prompt'

def setup_inputs():

    _w = (lambda a: (lambda t: t.to(torch.bfloat16) if t.is_floating_point() else t)(torch.as_tensor(a)))
    def _A_setup():
        B, S, D, H = 1, 2048, 576, 32
        Dh = D // H
        return {
            "x": _w((lambda *s: torch.randn(*s, dtype=torch.bfloat16) * 0.4)(B, S, D)),
            "wqkv": _w((lambda *s: torch.randn(*s, dtype=torch.bfloat16) * 1.6)(D, 3 * D)),
            "w_phi": _w((lambda *s: torch.randn(*s, dtype=torch.bfloat16) * 1.6)(H, Dh, Dh) * (Dh ** -0.5)),
            "wo": _w((lambda *s: torch.randn(*s, dtype=torch.bfloat16) * 1.6)(D, D)),
        }
    _A_in = _A_setup()

    def _B_setup():
        B, S, D = 1, 2048, 576
        ns = torch.arange(S)[:, None] * torch.arange(S)[None, :]
        nd = torch.arange(D)[:, None] * torch.arange(D)[None, :]
        Ws_re = torch.cos(-2.0 * torch.pi * ns / S).to(float)
        Ws_im = torch.sin(-2.0 * torch.pi * ns / S).to(float)
        Wd_re = torch.cos(-2.0 * torch.pi * nd / D).to(float)
        Wd_im = torch.sin(-2.0 * torch.pi * nd / D).to(float)
        return {
            "x": _w((lambda *s: torch.randn(*s, dtype=torch.bfloat16) * 0.4)(B, S, D)),
            "dft_seq_re": _w(Ws_re),
            "dft_seq_im": _w(Ws_im),
            "dft_hid_re": _w(Wd_re),
            "dft_hid_im": _w(Wd_im),
        }
    _B_in = _B_setup()

    return {
        "A_x": _A_in["x"],
        "A_wqkv": _A_in["wqkv"],
        "A_w_phi": _A_in["w_phi"],
        "A_wo": _A_in["wo"],
        "B_x": _B_in["x"],
        "B_dft_seq_re": _B_in["dft_seq_re"],
        "B_dft_seq_im": _B_in["dft_seq_im"],
        "B_dft_hid_re": _B_in["dft_hid_re"],
        "B_dft_hid_im": _B_in["dft_hid_im"],
    }

def reference(A_x, A_wqkv, A_w_phi, A_wo, B_x, B_dft_seq_re, B_dft_seq_im, B_dft_hid_re, B_dft_hid_im):
    def _A_ref(x, wqkv, w_phi, wo):
        B, S, D = x.shape
        H = 32; Dh = D // H
        qkv = (lambda x, *d: x.permute(*d))((x @ wqkv).reshape(B, S, 3, H, Dh), 2, 0, 3, 1, 4)
        q, k, v = qkv[0], qkv[1], qkv[2]
        pq = torch.einsum("bhsd,hdf->bhsf", q, w_phi)
        pk = torch.einsum("bhsd,hdf->bhsf", k, w_phi)
        phi_q = torch.concatenate([_softmax(pq, axis=-1), _softmax(-pq, axis=-1)], axis=-1)
        phi_k = torch.concatenate([_softmax(pk, axis=-1), _softmax(-pk, axis=-1)], axis=-1)
        kv = torch.einsum("bhsf,bhsd->bhfd", phi_k, v)
        z  = torch.einsum("bhsf->bhf", phi_k)
        num = torch.einsum("bhqf,bhfd->bhqd", phi_q, kv)
        den = torch.einsum("bhqf,bhf->bhq", phi_q, z)[..., None] + 1e-6
        o = num / den
        return (lambda x, *d: x.permute(*d))(o, 0, 2, 1, 3).reshape(B, S, D) @ wo

    def _B_ref(x, dft_seq_re, dft_seq_im, dft_hid_re, dft_hid_im):
        a = torch.einsum("bsd,ed->bse", x, dft_hid_re)
        b = torch.einsum("bsd,ed->bse", x, dft_hid_im)
        y = torch.einsum("ts,bse->bte", dft_seq_re, a) - torch.einsum("ts,bse->bte", dft_seq_im, b)
        return y

    x = A_x
    x = x + _A_ref(_layernorm(x, axis=-1), A_wqkv, A_w_phi, A_wo)
    x = x + _B_ref(_layernorm(x, axis=-1), B_dft_seq_re, B_dft_seq_im, B_dft_hid_re, B_dft_hid_im)
    x = x + _A_ref(_layernorm(x, axis=-1), A_wqkv, A_w_phi, A_wo)
    x = x + _B_ref(_layernorm(x, axis=-1), B_dft_seq_re, B_dft_seq_im, B_dft_hid_re, B_dft_hid_im)
    return x
```

```cpp
#include <hip/hip_runtime.h>
#include <hip/hip_bf16.h>
#include <stdint.h>

typedef unsigned short u16;
typedef __attribute__((ext_vector_type(8))) short bf16x8;
typedef __attribute__((ext_vector_type(4))) short s16x4;
typedef __attribute__((ext_vector_type(4))) float f32x4;
typedef __attribute__((ext_vector_type(4))) unsigned u32x4;
#define LDS_PTR(T) __attribute__((address_space(3))) T*

constexpr int S_ = 2048, D_ = 576, H_ = 32, DH_ = 18;
constexpr int PHS = 40;

__device__ __forceinline__ u16 f2bf(float f) {
    __hip_bfloat16 h = __float2bfloat16(f);
    return *reinterpret_cast<u16*>(&h);
}
__device__ __forceinline__ float bf2f(u16 u) { return __uint_as_float(((unsigned)u) << 16); }
__device__ __forceinline__ float rbf(float f) { return bf2f(f2bf(f)); }
__device__ __forceinline__ f32x4 mfma16(bf16x8 a, bf16x8 b, f32x4 c) {
    return __builtin_amdgcn_mfma_f32_16x16x32_bf16(a, b, c, 0, 0, 0);
}
__device__ __forceinline__ bf16x8 trfrag(const char* base, int rs, int lane) {
    int g = lane >> 4, q = (lane >> 2) & 3, p = lane & 3;
    const char* a0 = base + (8 * g + q) * rs + p * 8;
    s16x4 lo = __builtin_amdgcn_ds_read_tr16_b64_v4i16((LDS_PTR(s16x4))(a0));
    s16x4 hi = __builtin_amdgcn_ds_read_tr16_b64_v4i16((LDS_PTR(s16x4))(a0 + 4 * rs));
    bf16x8 r;
    r[0] = lo[0]; r[1] = lo[1]; r[2] = lo[2]; r[3] = lo[3];
    r[4] = hi[0]; r[5] = hi[1]; r[6] = hi[2]; r[7] = hi[3];
    return r;
}

__device__ __forceinline__ void ln_rows_to_lds(const u16* __restrict__ x, int row0, int nrows, char* lds, int lda, int tid) {
    for (int pass = 0; pass < nrows / 32; ++pass) {
        int r = pass * 32 + (tid >> 3), l8 = tid & 7;
        const u32x4* src = reinterpret_cast<const u32x4*>(x + (size_t)(row0 + r) * D_);
        u32x4 v[9];
#pragma unroll
        for (int i = 0; i < 9; ++i) v[i] = src[l8 + 8 * i];
        float s = 0.f;
#pragma unroll
        for (int i = 0; i < 9; ++i)
#pragma unroll
            for (int j = 0; j < 4; ++j) s += __uint_as_float(v[i][j] << 16) + __uint_as_float(v[i][j] & 0xffff0000u);
        s += __shfl_xor(s, 1); s += __shfl_xor(s, 2); s += __shfl_xor(s, 4);
        float mean = s * (1.0f / D_);
        float q = 0.f;
#pragma unroll
        for (int i = 0; i < 9; ++i)
#pragma unroll
            for (int j = 0; j < 4; ++j) {
                float a = __uint_as_float(v[i][j] << 16) - mean, b = __uint_as_float(v[i][j] & 0xffff0000u) - mean;
                q += a * a + b * b;
            }
        q += __shfl_xor(q, 1); q += __shfl_xor(q, 2); q += __shfl_xor(q, 4);
        float var = q * (1.0f / D_);
        float mb = rbf(mean);
        float sd = rbf(sqrtf(rbf(rbf(var) + 1e-5f)));
#pragma unroll
        for (int i = 0; i < 9; ++i) {
            u32x4 o;
#pragma unroll
            for (int j = 0; j < 4; ++j) {
                float a = rbf(__uint_as_float(v[i][j] << 16) - mb), b = rbf(__uint_as_float(v[i][j] & 0xffff0000u) - mb);
                o[j] = (unsigned)f2bf(a / sd) | ((unsigned)f2bf(b / sd) << 16);
            }
            *reinterpret_cast<u32x4*>(lds + r * lda + (l8 + 8 * i) * 16) = o;
        }
    }
}

constexpr int A1_LDA = 1168;
constexpr int A1_BB = 32 * 432 + 16;
constexpr int A1_LDS = 131072;
__global__ __launch_bounds__(256) void k_a1(const u16* __restrict__ x, const u16* __restrict__ wqkv,
                                            const u16* __restrict__ wphi, u16* __restrict__ phiq,
                                            float* __restrict__ part) {
    extern __shared__ __attribute__((aligned(16))) char smem[];
    const int tid = threadIdx.x, lane = tid & 63, wid = tid >> 6;
    const int hg = blockIdx.x & 7, mb = blockIdx.x >> 3;
    char* sA = smem;
    char* sB = smem + 64 * A1_LDA;
    auto stage = [&](int ks, int buf) {
        char* dst = sB + buf * A1_BB;
#pragma unroll
        for (int i = 0; i < 4; ++i) {
            int p = tid + 256 * i;
            if (p < 864) {
                int k = p / 27, sp = p % 27, seg = sp / 9, w = sp % 9;
                u32x4 v = *reinterpret_cast<const u32x4*>(wqkv + (size_t)(ks * 32 + k) * 1728 + seg * 576 + hg * 72 + w * 8);
                *reinterpret_cast<u32x4*>(dst + p * 16) = v;
            }
        }
    };
    stage(0, 0);
    ln_rows_to_lds(x, mb * 64, 64, sA, A1_LDA, tid);
    __syncthreads();
    const int wm = wid >> 1, wn = wid & 1;
    f32x4 acc[2][7];
#pragma unroll
    for (int m = 0; m < 2; ++m)
#pragma unroll
        for (int n = 0; n < 7; ++n) acc[m][n] = f32x4{0.f, 0.f, 0.f, 0.f};
    for (int ks = 0; ks < 18; ++ks) {
        if (ks + 1 < 18) stage(ks + 1, (ks + 1) & 1);
        const char* bb = sB + (ks & 1) * A1_BB;
        bf16x8 af[2];
#pragma unroll
        for (int m = 0; m < 2; ++m)
            af[m] = *reinterpret_cast<const bf16x8*>(sA + (wm * 32 + m * 16 + (lane & 15)) * A1_LDA + ks * 64 + (lane >> 4) * 16);
#pragma unroll
        for (int n = 0; n < 7; ++n) {
            bf16x8 bf = trfrag(bb + (wn * 7 + n) * 32, 432, lane);
#pragma unroll
            for (int m = 0; m < 2; ++m) acc[m][n] = mfma16(af[m], bf, acc[m][n]);
        }
        __syncthreads();
    }
    char* Cq = smem;
    float* P = reinterpret_cast<float*>(smem + 49152);
    u16* KT = reinterpret_cast<u16*>(smem + 90112);
    u16* VT = reinterpret_cast<u16*>(smem + 114688);
    {
        u32x4 z = {0u, 0u, 0u, 0u};
        for (int i = tid; i < 49152 / 16; i += 256) *reinterpret_cast<u32x4*>(Cq + i * 16) = z;
        for (int i = tid; i < 40960 / 16; i += 256) *reinterpret_cast<u32x4*>(smem + 90112 + i * 16) = z;
    }
    __syncthreads();
#pragma unroll
    for (int m = 0; m < 2; ++m)
#pragma unroll
        for (int n = 0; n < 7; ++n) {
            int c = (wn * 7 + n) * 16 + (lane & 15);
            if (c < 216) {
                int seg = c / 72, rem = c % 72, h = rem / 18, d = rem % 18;
#pragma unroll
                for (int r = 0; r < 4; ++r) {
                    int row = wm * 32 + m * 16 + (lane >> 4) * 4 + r;
                    *reinterpret_cast<u16*>(Cq + row * 768 + seg * 256 + h * 64 + d * 2) = f2bf(acc[m][n][r]);
                }
            }
        }
    __syncthreads();
    {
        const int h = wid, hglob = hg * 4 + h;
        bf16x8 wb[2];
#pragma unroll
        for (int ft = 0; ft < 2; ++ft)
#pragma unroll
            for (int j = 0; j < 8; ++j) {
                int k = (lane >> 4) * 8 + j, f = ft * 16 + (lane & 15);
                wb[ft][j] = (k < 18 && f < 18) ? (short)wphi[(size_t)hglob * 324 + k * 18 + f] : (short)0;
            }
#pragma unroll
        for (int seg = 0; seg < 2; ++seg)
#pragma unroll
            for (int mt = 0; mt < 4; ++mt) {
                bf16x8 a = *reinterpret_cast<const bf16x8*>(Cq + (mt * 16 + (lane & 15)) * 768 + seg * 256 + h * 64 + (lane >> 4) * 16);
#pragma unroll
                for (int ft = 0; ft < 2; ++ft) {
                    f32x4 c = mfma16(a, wb[ft], f32x4{0.f, 0.f, 0.f, 0.f});
                    int f = ft * 16 + (lane & 15);
                    if (f < 18) {
#pragma unroll
                        for (int r = 0; r < 4; ++r) {
                            int row = mt * 16 + (lane >> 4) * 4 + r;
                            P[((row * 2 + seg) * 4 + h) * 20 + f] = rbf(c[r]);
                        }
                    }
                }
            }
    }
    __syncthreads();
    {
        const int row = tid >> 2, h = tid & 3, hglob = hg * 4 + h;
#pragma unroll
        for (int seg = 0; seg < 2; ++seg) {
            float p[18];
            float mx = -INFINITY, mn = INFINITY;
#pragma unroll
            for (int f = 0; f < 18; ++f) { p[f] = P[((row * 2 + seg) * 4 + h) * 20 + f]; mx = fmaxf(mx, p[f]); mn = fminf(mn, p[f]); }
            float e1[18], e2[18], s1 = 0.f, s2 = 0.f;
#pragma unroll
            for (int f = 0; f < 18; ++f) { e1[f] = expf(p[f] - mx); e2[f] = expf(mn - p[f]); s1 += e1[f]; s2 += e2[f]; }
            u16 ph[36];
#pragma unroll
            for (int f = 0; f < 18; ++f) { ph[f] = f2bf(e1[f] / s1); ph[18 + f] = f2bf(e2[f] / s2); }
            if (seg == 0) {
                u16* dst = phiq + ((size_t)(mb * 64 + row) * H_ + hglob) * PHS;
#pragma unroll
                for (int f = 0; f < 36; f += 2) *reinterpret_cast<unsigned*>(dst + f) = (unsigned)ph[f] | ((unsigned)ph[f + 1] << 16);
                *reinterpret_cast<unsigned*>(dst + 36) = 0u;
                *reinterpret_cast<unsigned*>(dst + 38) = 0u;
            } else {
#pragma unroll
                for (int f = 0; f < 36; ++f) KT[(h * 48 + f) * 64 + row] = ph[f];
            }
        }
#pragma unroll
        for (int d = 0; d < 18; ++d) VT[(h * 32 + d) * 64 + row] = *reinterpret_cast<const u16*>(Cq + row * 768 + 2 * 256 + h * 64 + d * 2);
        VT[(h * 32 + 18) * 64 + row] = (u16)0x3F80;
    }
    __syncthreads();
    {
        const int h = wid, hglob = hg * 4 + h;
#pragma unroll
        for (int ft = 0; ft < 3; ++ft)
#pragma unroll
            for (int dt = 0; dt < 2; ++dt) {
                f32x4 c = f32x4{0.f, 0.f, 0.f, 0.f};
#pragma unroll
                for (int ks = 0; ks < 2; ++ks) {
                    bf16x8 a = *reinterpret_cast<const bf16x8*>(KT + (h * 48 + ft * 16 + (lane & 15)) * 64 + ks * 32 + (lane >> 4) * 8);
                    bf16x8 b = *reinterpret_cast<const bf16x8*>(VT + (h * 32 + dt * 16 + (lane & 15)) * 64 + ks * 32 + (lane >> 4) * 8);
                    c = mfma16(a, b, c);
                }
                int d = dt * 16 + (lane & 15);
                if (d < 20) {
#pragma unroll
                    for (int r = 0; r < 4; ++r) {
                        int f = ft * 16 + (lane >> 4) * 4 + r;
                        if (f < 36) part[(((size_t)mb * H_ + hglob) * 36 + f) * 20 + d] = c[r];
                    }
                }
            }
    }
}

__global__ __launch_bounds__(256) void k_o(const u16* __restrict__ phiq, const float* __restrict__ part, u16* __restrict__ o) {
    __shared__ __attribute__((aligned(16))) u16 KV[32 * 64];
    const int tid = threadIdx.x, lane = tid & 63, wid = tid >> 6;
    const int h = blockIdx.x >> 3, rc = blockIdx.x & 7;
    for (int i = tid; i < 32 * 64; i += 256) KV[i] = 0;
    __syncthreads();
    for (int i = tid; i < 720; i += 256) {
        int f = i / 20, d = i % 20;
        if (d < 19) {
            float s = 0.f;
#pragma unroll 8
            for (int m = 0; m < 32; ++m) s += part[((size_t)m * H_ + h) * 720 + i];
            KV[d * 64 + f] = f2bf(s);
        }
    }
    __syncthreads();
    bf16x8 bfr[2][2];
#pragma unroll
    for (int dt = 0; dt < 2; ++dt)
#pragma unroll
        for (int ks = 0; ks < 2; ++ks)
            bfr[dt][ks] = *reinterpret_cast<const bf16x8*>(KV + (dt * 16 + (lane & 15)) * 64 + ks * 32 + (lane >> 4) * 8);
#pragma unroll
    for (int mt = 0; mt < 4; ++mt) {
        int row0 = rc * 256 + wid * 64 + mt * 16;
        const u16* src = phiq + ((size_t)(row0 + (lane & 15)) * H_ + h) * PHS;
        bf16x8 a0 = *reinterpret_cast<const bf16x8*>(src + (lane >> 4) * 8);
        bf16x8 a1;
        if ((lane >> 4) == 0) a1 = *reinterpret_cast<const bf16x8*>(src + 32);
        else a1 = bf16x8{0, 0, 0, 0, 0, 0, 0, 0};
        f32x4 c0 = f32x4{0.f, 0.f, 0.f, 0.f}, c1 = c0;
        c0 = mfma16(a0, bfr[0][0], c0); c0 = mfma16(a1, bfr[0][1], c0);
        c1 = mfma16(a0, bfr[1][0], c1); c1 = mfma16(a1, bfr[1][1], c1);
#pragma unroll
        for (int r = 0; r < 4; ++r) {
            float den = __shfl(c1[r], (lane & 48) + 2);
            den = rbf(rbf(den) + 1e-6f);
            int row = row0 + (lane >> 4) * 4 + r;
            o[(size_t)row * D_ + h * 18 + (lane & 15)] = f2bf(rbf(c0[r]) / den);
            if ((lane & 15) < 2) o[(size_t)row * D_ + h * 18 + 16 + (lane & 15)] = f2bf(rbf(c1[r]) / den);
        }
    }
}

constexpr int A2_BB = 32 * 288;
__global__ __launch_bounds__(384) void k_a2(const u16* __restrict__ o, const u16* __restrict__ wo,
                                            const u16* __restrict__ x, u16* __restrict__ xo) {
    __shared__ __attribute__((aligned(16))) char smem[32 * A1_LDA + 2 * A2_BB];
    const int tid = threadIdx.x, lane = tid & 63, wid = tid >> 6;
    const int nt4 = blockIdx.x & 3, mt = blockIdx.x >> 2;
    char* sA = smem;
    char* sB = smem + 32 * A1_LDA;
    for (int p = tid; p < 32 * 72; p += 384) {
        int r = p / 72, c = p % 72;
        *reinterpret_cast<u32x4*>(sA + r * A1_LDA + c * 16) = *reinterpret_cast<const u32x4*>(o + (size_t)(mt * 32 + r) * D_ + c * 8);
    }
    auto stage = [&](int ks, int buf) {
        for (int p = tid; p < 32 * 18; p += 384) {
            int k = p / 18, c = p % 18;
            *reinterpret_cast<u32x4*>(sB + buf * A2_BB + p * 16) =
                *reinterpret_cast<const u32x4*>(wo + (size_t)(ks * 32 + k) * D_ + nt4 * 144 + c * 8);
        }
    };
    stage(0, 0);
    __syncthreads();
    const int wm = wid / 3, wn = wid % 3;
    f32x4 acc[3];
#pragma unroll
    for (int n = 0; n < 3; ++n) acc[n] = f32x4{0.f, 0.f, 0.f, 0.f};
    for (int ks = 0; ks < 18; ++ks) {
        if (ks + 1 < 18) stage(ks + 1, (ks + 1) & 1);
        const char* bb = sB + (ks & 1) * A2_BB;
        bf16x8 af = *reinterpret_cast<const bf16x8*>(sA + (wm * 16 + (lane & 15)) * A1_LDA + ks * 64 + (lane >> 4) * 16);
#pragma unroll
        for (int n = 0; n < 3; ++n) {
            bf16x8 bf = trfrag(bb + (wn * 3 + n) * 32, 288, lane);
            acc[n] = mfma16(af, bf, acc[n]);
        }
        __syncthreads();
    }
#pragma unroll
    for (int n = 0; n < 3; ++n)
#pragma unroll
        for (int r = 0; r < 4; ++r) {
            int row = mt * 32 + wm * 16 + (lane >> 4) * 4 + r, col = nt4 * 144 + (wn * 3 + n) * 16 + (lane & 15);
            size_t idx = (size_t)row * D_ + col;
            xo[idx] = f2bf(bf2f(x[idx]) + rbf(acc[n][r]));
        }
}

constexpr int B1_BB = 144 * 64 + 0;
__global__ __launch_bounds__(384) void k_b1(const u16* __restrict__ x, const u16* __restrict__ wre,
                                            const u16* __restrict__ wim, u16* __restrict__ bt) {
    extern __shared__ __attribute__((aligned(16))) char smem[];
    const int tid = threadIdx.x, lane = tid & 63, wid = tid >> 6;
    const int nb = blockIdx.x & 7, mb = blockIdx.x >> 3;
    const int partn = nb >> 2, e0 = (nb & 3) * 144;
    const u16* W = partn ? wim : wre;
    char* sA = smem;
    char* sB = smem + 64 * A1_LDA;
    auto stage = [&](int ks, int buf) {
        for (int p = tid; p < 144 * 4; p += 384) {
            int n = p >> 2, c = p & 3;
            *reinterpret_cast<u32x4*>(sB + buf * B1_BB + n * 64 + ((c ^ (n & 3)) * 16)) =
                *reinterpret_cast<const u32x4*>(W + (size_t)(e0 + n) * D_ + ks * 32 + c * 8);
        }
    };
    stage(0, 0);
    if (tid < 256) ln_rows_to_lds(x, mb * 64, 64, sA, A1_LDA, tid);
    __syncthreads();
    const int wm = wid / 3, wn = wid % 3;
    f32x4 acc[2][3];
#pragma unroll
    for (int m = 0; m < 2; ++m)
#pragma unroll
        for (int n = 0; n < 3; ++n) acc[m][n] = f32x4{0.f, 0.f, 0.f, 0.f};
    for (int ks = 0; ks < 18; ++ks) {
        if (ks + 1 < 18) stage(ks + 1, (ks + 1) & 1);
        const char* bb = sB + (ks & 1) * B1_BB;
        bf16x8 af[2];
#pragma unroll
        for (int m = 0; m < 2; ++m)
            af[m] = *reinterpret_cast<const bf16x8*>(sA + (wm * 32 + m * 16 + (lane & 15)) * A1_LDA + ks * 64 + (lane >> 4) * 16);
#pragma unroll
        for (int n = 0; n < 3; ++n) {
            int nn = (wn * 3 + n) * 16 + (lane & 15);
            bf16x8 bf = *reinterpret_cast<const bf16x8*>(bb + nn * 64 + (((lane >> 4) ^ (nn & 3)) * 16));
#pragma unroll
            for (int m = 0; m < 2; ++m) acc[m][n] = mfma16(af[m], bf, acc[m][n]);
        }
        __syncthreads();
    }
#pragma unroll
    for (int m = 0; m < 2; ++m)
#pragma unroll
        for (int n = 0; n < 3; ++n) {
            int e = e0 + (wn * 3 + n) * 16 + (lane & 15);
            int s = mb * 64 + wm * 32 + m * 16 + (lane >> 4) * 4;
            s16x4 v;
#pragma unroll
            for (int r = 0; r < 4; ++r) v[r] = (short)f2bf(acc[m][n][r]);
            *reinterpret_cast<s16x4*>(bt + ((size_t)partn * D_ + e) * S_ + s) = v;
        }
}

constexpr int B2_ST = 45056;
__global__ __launch_bounds__(256) void k_b2(const u16* __restrict__ cre, const u16* __restrict__ cim,
                                            const u16* __restrict__ bt, const u16* __restrict__ x, u16* __restrict__ xo) {
    extern __shared__ __attribute__((aligned(16))) char smem[];
    const int tid = threadIdx.x, lane = tid & 63, wid = tid >> 6;
    const int xcd = blockIdx.x & 7, idx = blockIdx.x >> 3;
    const int mt = xcd * 8 + (idx >> 2), nt4 = idx & 3;
    const int t0 = mt * 32, n0 = nt4 * 144;
    const u16* src[11];
#pragma unroll
    for (int i = 0; i < 11; ++i) {
        int P = i * 256 + tid;
        if (i < 2) {
            int row = (P & 255) >> 3, c = (P & 7) ^ (row & 7);
            src[i] = (i == 0 ? cre : cim) + (size_t)(t0 + row) * S_ + c * 8;
        } else {
            int PB = P - 512, pp = PB / 1152, row = (PB % 1152) >> 3, c = (PB & 7) ^ (row & 7);
            src[i] = bt + ((size_t)pp * D_ + n0 + row) * S_ + c * 8;
        }
    }
    auto issue = [&](int it, int buf) {
        char* dst = smem + buf * B2_ST + (wid * 64) * 16;
#pragma unroll
        for (int i = 0; i < 11; ++i)
            __builtin_amdgcn_global_load_lds((const unsigned*)(src[i] + it * 64), (LDS_PTR(unsigned))(dst + i * 4096), 16, 0, 0);
    };
    const int part = wid >> 1, kh = wid & 1;
    f32x4 acc[2][9];
#pragma unroll
    for (int m = 0; m < 2; ++m)
#pragma unroll
        for (int n = 0; n < 9; ++n) acc[m][n] = f32x4{0.f, 0.f, 0.f, 0.f};
    issue(0, 0); issue(1, 1);
    for (int it = 0; it < 32; ++it) {
        if (it + 1 < 32) asm volatile("s_waitcnt vmcnt(11)" ::: "memory");
        else asm volatile("s_waitcnt vmcnt(0)" ::: "memory");
        asm volatile("s_waitcnt lgkmcnt(0)" ::: "memory");
        __builtin_amdgcn_s_barrier();
        if (it + 2 < 32) issue(it + 2, (it + 2) % 3);
        const char* sb = smem + (it % 3) * B2_ST;
        const char* sa = sb + part * 4096;
        const char* sbb = sb + 8192 + part * 18432;
        bf16x8 af[2];
#pragma unroll
        for (int m = 0; m < 2; ++m) {
            int row = m * 16 + (lane & 15), c = kh * 4 + (lane >> 4);
            af[m] = *reinterpret_cast<const bf16x8*>(sa + row * 128 + ((c ^ (row & 7)) * 16));
        }
#pragma unroll
        for (int n = 0; n < 9; ++n) {
            int row = n * 16 + (lane & 15), c = kh * 4 + (lane >> 4);
            bf16x8 bf = *reinterpret_cast<const bf16x8*>(sbb + row * 128 + ((c ^ (row & 7)) * 16));
#pragma unroll
            for (int m = 0; m < 2; ++m) acc[m][n] = mfma16(af[m], bf, acc[m][n]);
        }
    }
    __syncthreads();
    float* red = reinterpret_cast<float*>(smem);
    if (kh == 1) {
#pragma unroll
        for (int m = 0; m < 2; ++m)
#pragma unroll
            for (int n = 0; n < 9; ++n)
#pragma unroll
                for (int r = 0; r < 4; ++r) red[wid * 4608 + ((m * 9 + n) * 4 + r) * 64 + lane] = acc[m][n][r];
    }
    __syncthreads();
    if (kh == 0) {
#pragma unroll
        for (int m = 0; m < 2; ++m)
#pragma unroll
            for (int n = 0; n < 9; ++n)
#pragma unroll
                for (int r = 0; r < 4; ++r) acc[m][n][r] = rbf(acc[m][n][r] + red[(wid + 1) * 4608 + ((m * 9 + n) * 4 + r) * 64 + lane]);
        if (wid == 2) {
#pragma unroll
            for (int m = 0; m < 2; ++m)
#pragma unroll
                for (int n = 0; n < 9; ++n)
#pragma unroll
                    for (int r = 0; r < 4; ++r) red[((m * 9 + n) * 4 + r) * 64 + lane] = acc[m][n][r];
        }
    }
    __syncthreads();
    if (wid == 0) {
#pragma unroll
        for (int m = 0; m < 2; ++m)
#pragma unroll
            for (int n = 0; n < 9; ++n)
#pragma unroll
                for (int r = 0; r < 4; ++r) {
                    float y = rbf(acc[m][n][r] - red[((m * 9 + n) * 4 + r) * 64 + lane]);
                    int row = t0 + m * 16 + (lane >> 4) * 4 + r, col = n0 + n * 16 + (lane & 15);
                    size_t gi = (size_t)row * D_ + col;
                    xo[gi] = f2bf(bf2f(x[gi]) + y);
                }
    }
}

extern "C" void kernel_launch(void* const* d_in, const int* in_sizes, int n_in, void* d_out, int out_size,
                              void* d_ws, size_t ws_size, hipStream_t stream) {
    const u16* A_x = (const u16*)d_in[0];
    const u16* wqkv = (const u16*)d_in[1];
    const u16* wphi = (const u16*)d_in[2];
    const u16* wo = (const u16*)d_in[3];
    const u16* cre = (const u16*)d_in[5];
    const u16* cim = (const u16*)d_in[6];
    const u16* dre = (const u16*)d_in[7];
    const u16* dim_ = (const u16*)d_in[8];
    char* ws = (char*)d_ws;
    const size_t XB = (size_t)S_ * D_ * 2;
    u16* X1 = (u16*)(ws);
    u16* X2 = (u16*)(ws + XB);
    u16* X3 = (u16*)(ws + 2 * XB);
    u16* O = (u16*)(ws + 3 * XB);
    u16* PHIQ = (u16*)(ws + 4 * XB);
    float* PART = (float*)(ws + 4 * XB + (size_t)S_ * H_ * PHS * 2);
    u16* BT = (u16*)(ws + 4 * XB + (size_t)S_ * H_ * PHS * 2 + (size_t)32 * H_ * 720 * 4);
    u16* XOUT = (u16*)d_out;

    hipFuncSetAttribute((const void*)k_a1, hipFuncAttributeMaxDynamicSharedMemorySize, A1_LDS);
    hipFuncSetAttribute((const void*)k_b1, hipFuncAttributeMaxDynamicSharedMemorySize, 64 * A1_LDA + 2 * B1_BB);
    hipFuncSetAttribute((const void*)k_b2, hipFuncAttributeMaxDynamicSharedMemorySize, 3 * B2_ST);
    const u16* xin = A_x;
    u16* xs[4] = {X1, X2, X3, XOUT};
    for (int layer = 0; layer < 4; ++layer) {
        u16* xo = xs[layer];
        if ((layer & 1) == 0) {
            k_a1<<<256, 256, A1_LDS, stream>>>(xin, wqkv, wphi, PHIQ, PART);
            k_o<<<256, 256, 0, stream>>>(PHIQ, PART, O);
            k_a2<<<256, 384, 0, stream>>>(O, wo, xin, xo);
        } else {
            k_b1<<<256, 384, 64 * A1_LDA + 2 * B1_BB, stream>>>(xin, dre, dim_, BT);
            k_b2<<<256, 256, 3 * B2_ST, stream>>>(cre, cim, BT, xin, xo);
        }
        xin = xo;
    }
}
```

```cpp
#include <hip/hip_runtime.h>
#include <hip/hip_bf16.h>
#include <stdint.h>

typedef unsigned short u16;
typedef __attribute__((ext_vector_type(8))) short bf16x8;
typedef __attribute__((ext_vector_type(4))) short s16x4;
typedef __attribute__((ext_vector_type(4))) float f32x4;
typedef __attribute__((ext_vector_type(4))) unsigned u32x4;
#define LDS_PTR(T) __attribute__((address_space(3))) T*

constexpr int S_ = 2048, D_ = 576, H_ = 32, DH_ = 18;
constexpr int PHS = 40;

__device__ __forceinline__ u16 f2bf(float f) {
    __hip_bfloat16 h = __float2bfloat16(f);
    return *reinterpret_cast<u16*>(&h);
}
__device__ __forceinline__ float bf2f(u16 u) { return __uint_as_float(((unsigned)u) << 16); }
__device__ __forceinline__ float rbf(float f) { return bf2f(f2bf(f)); }
__device__ __forceinline__ f32x4 mfma16(bf16x8 a, bf16x8 b, f32x4 c) {
    return __builtin_amdgcn_mfma_f32_16x16x32_bf16(a, b, c, 0, 0, 0);
}
__device__ __forceinline__ bf16x8 trfrag(const char* base, int rs, int lane) {
    int g = lane >> 4, q = (lane >> 2) & 3, p = lane & 3;
    const char* a0 = base + (8 * g + q) * rs + p * 8;
    s16x4 lo = __builtin_amdgcn_ds_read_tr16_b64_v4i16((LDS_PTR(s16x4))(a0));
    s16x4 hi = __builtin_amdgcn_ds_read_tr16_b64_v4i16((LDS_PTR(s16x4))(a0 + 4 * rs));
    bf16x8 r;
    r[0] = lo[0]; r[1] = lo[1]; r[2] = lo[2]; r[3] = lo[3];
    r[4] = hi[0]; r[5] = hi[1]; r[6] = hi[2]; r[7] = hi[3];
    return r;
}

__device__ __forceinline__ void ln_rows_to_lds(const u16* __restrict__ x, int row0, int nrows, char* lds, int lda, int tid) {
    for (int pass = 0; pass < nrows / 32; ++pass) {
        int r = pass * 32 + (tid >> 3), l8 = tid & 7;
        const u32x4* src = reinterpret_cast<const u32x4*>(x + (size_t)(row0 + r) * D_);
        u32x4 v[9];
#pragma unroll
        for (int i = 0; i < 9; ++i) v[i] = src[l8 + 8 * i];
        float s = 0.f;
#pragma unroll
        for (int i = 0; i < 9; ++i)
#pragma unroll
            for (int j = 0; j < 4; ++j) s += __uint_as_float(v[i][j] << 16) + __uint_as_float(v[i][j] & 0xffff0000u);
        s += __shfl_xor(s, 1); s += __shfl_xor(s, 2); s += __shfl_xor(s, 4);
        float mean = s * (1.0f / D_);
        float q = 0.f;
#pragma unroll
        for (int i = 0; i < 9; ++i)
#pragma unroll
            for (int j = 0; j < 4; ++j) {
                float a = __uint_as_float(v[i][j] << 16) - mean, b = __uint_as_float(v[i][j] & 0xffff0000u) - mean;
                q += a * a + b * b;
            }
        q += __shfl_xor(q, 1); q += __shfl_xor(q, 2); q += __shfl_xor(q, 4);
        float var = q * (1.0f / D_);
        float mb = rbf(mean);
        float sd = rbf(sqrtf(rbf(rbf(var) + 1e-5f)));
#pragma unroll
        for (int i = 0; i < 9; ++i) {
            u32x4 o;
#pragma unroll
            for (int j = 0; j < 4; ++j) {
                float a = rbf(__uint_as_float(v[i][j] << 16) - mb), b = rbf(__uint_as_float(v[i][j] & 0xffff0000u) - mb);
                o[j] = (unsigned)f2bf(a / sd) | ((unsigned)f2bf(b / sd) << 16);
            }
            *reinterpret_cast<u32x4*>(lds + r * lda + (l8 + 8 * i) * 16) = o;
        }
    }
}

constexpr int A1_LDA = 1168;
constexpr int A1_RS = 480;
constexpr int A1_BB = 16384;
constexpr int A1_LDS = 64 * A1_LDA + 4 * A1_BB;
__global__ __launch_bounds__(256) void k_a1(const u16* __restrict__ x, const u16* __restrict__ wqkv,
                                            const u16* __restrict__ wphi, u16* __restrict__ phiq,
                                            float* __restrict__ part) {
    extern __shared__ __attribute__((aligned(16))) char smem[];
    const int tid = threadIdx.x, lane = tid & 63, wid = tid >> 6;
    const int hg = blockIdx.x & 7, mb = blockIdx.x >> 3;
    char* sA = smem;
    char* sB = smem + 64 * A1_LDA;
    const u16* src[4];
#pragma unroll
    for (int i = 0; i < 4; ++i) {
        int p = tid + 256 * i;
        int k = p / 30, c = p % 30;
        if (k > 31) k = 31;
        if (c > 26) c = 26;
        int seg = c / 9, w = c % 9;
        src[i] = wqkv + (size_t)k * 1728 + seg * 576 + hg * 72 + w * 8;
    }
    auto issue = [&](int ks, int buf) {
        char* dst = sB + buf * A1_BB + wid * 1024;
#pragma unroll
        for (int i = 0; i < 4; ++i)
            __builtin_amdgcn_global_load_lds((const unsigned*)(src[i] + (size_t)ks * 32 * 1728), (LDS_PTR(unsigned))(dst + i * 4096), 16, 0, 0);
    };
    issue(0, 0); issue(1, 1); issue(2, 2);
    ln_rows_to_lds(x, mb * 64, 64, sA, A1_LDA, tid);
    const int wm = wid >> 1, wn = wid & 1;
    f32x4 acc[2][7];
#pragma unroll
    for (int m = 0; m < 2; ++m)
#pragma unroll
        for (int n = 0; n < 7; ++n) acc[m][n] = f32x4{0.f, 0.f, 0.f, 0.f};
    for (int ks = 0; ks < 18; ++ks) {
        if (ks < 16) asm volatile("s_waitcnt vmcnt(8)" ::: "memory");
        else if (ks == 16) asm volatile("s_waitcnt vmcnt(4)" ::: "memory");
        else asm volatile("s_waitcnt vmcnt(0)" ::: "memory");
        asm volatile("s_waitcnt lgkmcnt(0)" ::: "memory");
        __builtin_amdgcn_s_barrier();
        if (ks + 3 < 18) issue(ks + 3, (ks + 3) & 3);
        const char* bb = sB + (ks & 3) * A1_BB;
        bf16x8 af[2];
#pragma unroll
        for (int m = 0; m < 2; ++m)
            af[m] = *reinterpret_cast<const bf16x8*>(sA + (wm * 32 + m * 16 + (lane & 15)) * A1_LDA + ks * 64 + (lane >> 4) * 16);
#pragma unroll
        for (int n = 0; n < 7; ++n) {
            bf16x8 bf = trfrag(bb + (wn * 7 + n) * 32, A1_RS, lane);
#pragma unroll
            for (int m = 0; m < 2; ++m) acc[m][n] = mfma16(af[m], bf, acc[m][n]);
        }
    }
    __syncthreads();
    char* Cq = smem;
    float* P = reinterpret_cast<float*>(smem + 49152);
    u16* KT = reinterpret_cast<u16*>(smem + 90112);
    u16* VT = reinterpret_cast<u16*>(smem + 114688);
    {
        u32x4 z = {0u, 0u, 0u, 0u};
        for (int i = tid; i < 49152 / 16; i += 256) *reinterpret_cast<u32x4*>(Cq + i * 16) = z;
        for (int i = tid; i < 40960 / 16; i += 256) *reinterpret_cast<u32x4*>(smem + 90112 + i * 16) = z;
    }
    __syncthreads();
#pragma unroll
    for (int m = 0; m < 2; ++m)
#pragma unroll
        for (int n = 0; n < 7; ++n) {
            int c = (wn * 7 + n) * 16 + (lane & 15);
            if (c < 216) {
                int seg = c / 72, rem = c % 72, h = rem / 18, d = rem % 18;
#pragma unroll
                for (int r = 0; r < 4; ++r) {
                    int row = wm * 32 + m * 16 + (lane >> 4) * 4 + r;
                    *reinterpret_cast<u16*>(Cq + row * 768 + seg * 256 + h * 64 + d * 2) = f2bf(acc[m][n][r]);
                }
            }
        }
    __syncthreads();
    {
        const int h = wid, hglob = hg * 4 + h;
        bf16x8 wb[2];
#pragma unroll
        for (int ft = 0; ft < 2; ++ft)
#pragma unroll
            for (int j = 0; j < 8; ++j) {
                int k = (lane >> 4) * 8 + j, f = ft * 16 + (lane & 15);
                wb[ft][j] = (k < 18 && f < 18) ? (short)wphi[(size_t)hglob * 324 + k * 18 + f] : (short)0;
            }
#pragma unroll
        for (int seg = 0; seg < 2; ++seg)
#pragma unroll
            for (int mt = 0; mt < 4; ++mt) {
                bf16x8 a = *reinterpret_cast<const bf16x8*>(Cq + (mt * 16 + (lane & 15)) * 768 + seg * 256 + h * 64 + (lane >> 4) * 16);
#pragma unroll
                for (int ft = 0; ft < 2; ++ft) {
                    f32x4 c = mfma16(a, wb[ft], f32x4{0.f, 0.f, 0.f, 0.f});
                    int f = ft * 16 + (lane & 15);
                    if (f < 18) {
#pragma unroll
                        for (int r = 0; r < 4; ++r) {
                            int row = mt * 16 + (lane >> 4) * 4 + r;
                            P[((row * 2 + seg) * 4 + h) * 20 + f] = rbf(c[r]);
                        }
                    }
                }
            }
    }
    __syncthreads();
    {
        const int row = tid >> 2, h = tid & 3, hglob = hg * 4 + h;
#pragma unroll
        for (int seg = 0; seg < 2; ++seg) {
            float p[18];
            float mx = -INFINITY, mn = INFINITY;
#pragma unroll
            for (int f = 0; f < 18; ++f) { p[f] = P[((row * 2 + seg) * 4 + h) * 20 + f]; mx = fmaxf(mx, p[f]); mn = fminf(mn, p[f]); }
            float e1[18], e2[18], s1 = 0.f, s2 = 0.f;
#pragma unroll
            for (int f = 0; f < 18; ++f) { e1[f] = expf(p[f] - mx); e2[f] = expf(mn - p[f]); s1 += e1[f]; s2 += e2[f]; }
            u16 ph[36];
#pragma unroll
            for (int f = 0; f < 18; ++f) { ph[f] = f2bf(e1[f] / s1); ph[18 + f] = f2bf(e2[f] / s2); }
            if (seg == 0) {
                u16* dst = phiq + ((size_t)(mb * 64 + row) * H_ + hglob) * PHS;
#pragma unroll
                for (int f = 0; f < 36; f += 2) *reinterpret_cast<unsigned*>(dst + f) = (unsigned)ph[f] | ((unsigned)ph[f + 1] << 16);
                *reinterpret_cast<unsigned*>(dst + 36) = 0u;
                *reinterpret_cast<unsigned*>(dst + 38) = 0u;
            } else {
#pragma unroll
                for (int f = 0; f < 36; ++f) KT[(h * 48 + f) * 64 + row] = ph[f];
            }
        }
#pragma unroll
        for (int d = 0; d < 18; ++d) VT[(h * 32 + d) * 64 + row] = *reinterpret_cast<const u16*>(Cq + row * 768 + 2 * 256 + h * 64 + d * 2);
        VT[(h * 32 + 18) * 64 + row] = (u16)0x3F80;
    }
    __syncthreads();
    {
        const int h = wid, hglob = hg * 4 + h;
#pragma unroll
        for (int ft = 0; ft < 3; ++ft)
#pragma unroll
            for (int dt = 0; dt < 2; ++dt) {
                f32x4 c = f32x4{0.f, 0.f, 0.f, 0.f};
#pragma unroll
                for (int ks = 0; ks < 2; ++ks) {
                    bf16x8 a = *reinterpret_cast<const bf16x8*>(KT + (h * 48 + ft * 16 + (lane & 15)) * 64 + ks * 32 + (lane >> 4) * 8);
                    bf16x8 b = *reinterpret_cast<const bf16x8*>(VT + (h * 32 + dt * 16 + (lane & 15)) * 64 + ks * 32 + (lane >> 4) * 8);
                    c = mfma16(a, b, c);
                }
                int d = dt * 16 + (lane & 15);
                if (d < 20) {
#pragma unroll
                    for (int r = 0; r < 4; ++r) {
                        int f = ft * 16 + (lane >> 4) * 4 + r;
                        if (f < 36) part[(((size_t)mb * H_ + hglob) * 36 + f) * 20 + d] = c[r];
                    }
                }
            }
    }
}

__global__ __launch_bounds__(256) void k_o(const u16* __restrict__ phiq, const float* __restrict__ part, u16* __restrict__ o) {
    __shared__ __attribute__((aligned(16))) u16 KV[32 * 64];
    const int tid = threadIdx.x, lane = tid & 63, wid = tid >> 6;
    const int h = blockIdx.x >> 3, rc = blockIdx.x & 7;
    for (int i = tid; i < 32 * 64; i += 256) KV[i] = 0;
    __syncthreads();
    for (int i = tid; i < 720; i += 256) {
        int f = i / 20, d = i % 20;
        if (d < 19) {
            float v[32];
#pragma unroll
            for (int m = 0; m < 32; ++m) v[m] = part[((size_t)m * H_ + h) * 720 + i];
            float s = 0.f;
#pragma unroll
            for (int m = 0; m < 32; ++m) s += v[m];
            KV[d * 64 + f] = f2bf(s);
        }
    }
    __syncthreads();
    bf16x8 bfr[2][2];
#pragma unroll
    for (int dt = 0; dt < 2; ++dt)
#pragma unroll
        for (int ks = 0; ks < 2; ++ks)
            bfr[dt][ks] = *reinterpret_cast<const bf16x8*>(KV + (dt * 16 + (lane & 15)) * 64 + ks * 32 + (lane >> 4) * 8);
#pragma unroll
    for (int mt = 0; mt < 4; ++mt) {
        int row0 = rc * 256 + wid * 64 + mt * 16;
        const u16* src = phiq + ((size_t)(row0 + (lane & 15)) * H_ + h) * PHS;
        bf16x8 a0 = *reinterpret_cast<const bf16x8*>(src + (lane >> 4) * 8);
        bf16x8 a1;
        if ((lane >> 4) == 0) a1 = *reinterpret_cast<const bf16x8*>(src + 32);
        else a1 = bf16x8{0, 0, 0, 0, 0, 0, 0, 0};
        f32x4 c0 = f32x4{0.f, 0.f, 0.f, 0.f}, c1 = c0;
        c0 = mfma16(a0, bfr[0][0], c0); c0 = mfma16(a1, bfr[0][1], c0);
        c1 = mfma16(a0, bfr[1][0], c1); c1 = mfma16(a1, bfr[1][1], c1);
#pragma unroll
        for (int r = 0; r < 4; ++r) {
            float den = __shfl(c1[r], (lane & 48) + 2);
            den = rbf(rbf(den) + 1e-6f);
            int row = row0 + (lane >> 4) * 4 + r;
            o[(size_t)row * D_ + h * 18 + (lane & 15)] = f2bf(rbf(c0[r]) / den);
            if ((lane & 15) < 2) o[(size_t)row * D_ + h * 18 + 16 + (lane & 15)] = f2bf(rbf(c1[r]) / den);
        }
    }
}

constexpr int A2_BB = 64 * 288;
constexpr int A2_LDS = 32 * A1_LDA + 4 * A2_BB;
__global__ __launch_bounds__(384) void k_a2(const u16* __restrict__ o, const u16* __restrict__ wo,
                                            const u16* __restrict__ x, u16* __restrict__ xo) {
    extern __shared__ __attribute__((aligned(16))) char smem[];
    const int tid = threadIdx.x, lane = tid & 63, wid = tid >> 6;
    const int nt4 = blockIdx.x & 3, mt = blockIdx.x >> 2;
    char* sA = smem;
    char* sB = smem + 32 * A1_LDA;
    const u16* src[3];
#pragma unroll
    for (int i = 0; i < 3; ++i) {
        int p = tid + 384 * i, k = p / 18, c = p % 18;
        src[i] = wo + (size_t)k * D_ + nt4 * 144 + c * 8;
    }
    auto issue = [&](int ks, int buf) {
        char* dst = sB + buf * A2_BB + wid * 1024;
#pragma unroll
        for (int i = 0; i < 3; ++i)
            __builtin_amdgcn_global_load_lds((const unsigned*)(src[i] + (size_t)ks * 64 * D_), (LDS_PTR(unsigned))(dst + i * 6144), 16, 0, 0);
    };
    issue(0, 0); issue(1, 1); issue(2, 2);
    for (int p = tid; p < 32 * 72; p += 384) {
        int r = p / 72, c = p % 72;
        *reinterpret_cast<u32x4*>(sA + r * A1_LDA + c * 16) = *reinterpret_cast<const u32x4*>(o + (size_t)(mt * 32 + r) * D_ + c * 8);
    }
    const int wm = wid / 3, wn = wid % 3;
    f32x4 acc[3];
#pragma unroll
    for (int n = 0; n < 3; ++n) acc[n] = f32x4{0.f, 0.f, 0.f, 0.f};
    for (int ks = 0; ks < 9; ++ks) {
        if (ks < 7) asm volatile("s_waitcnt vmcnt(6)" ::: "memory");
        else if (ks == 7) asm volatile("s_waitcnt vmcnt(3)" ::: "memory");
        else asm volatile("s_waitcnt vmcnt(0)" ::: "memory");
        asm volatile("s_waitcnt lgkmcnt(0)" ::: "memory");
        __builtin_amdgcn_s_barrier();
        if (ks + 3 < 9) issue(ks + 3, (ks + 3) & 3);
        const char* bb = sB + (ks & 3) * A2_BB;
#pragma unroll
        for (int kk = 0; kk < 2; ++kk) {
            bf16x8 af = *reinterpret_cast<const bf16x8*>(sA + (wm * 16 + (lane & 15)) * A1_LDA + ks * 128 + kk * 64 + (lane >> 4) * 16);
#pragma unroll
            for (int n = 0; n < 3; ++n) {
                bf16x8 bf = trfrag(bb + kk * 32 * 288 + (wn * 3 + n) * 32, 288, lane);
                acc[n] = mfma16(af, bf, acc[n]);
            }
        }
    }
#pragma unroll
    for (int n = 0; n < 3; ++n)
#pragma unroll
        for (int r = 0; r < 4; ++r) {
            int row = mt * 32 + wm * 16 + (lane >> 4) * 4 + r, col = nt4 * 144 + (wn * 3 + n) * 16 + (lane & 15);
            size_t idx = (size_t)row * D_ + col;
            xo[idx] = f2bf(bf2f(x[idx]) + rbf(acc[n][r]));
        }
}

constexpr int B1_BB = 144 * 128;
constexpr int B1_LDS = 64 * A1_LDA + 4 * B1_BB;
__global__ __launch_bounds__(384) void k_b1(const u16* __restrict__ x, const u16* __restrict__ wre,
                                            const u16* __restrict__ wim, u16* __restrict__ bt) {
    extern __shared__ __attribute__((aligned(16))) char smem[];
    const int tid = threadIdx.x, lane = tid & 63, wid = tid >> 6;
    const int nb = blockIdx.x & 7, mb = blockIdx.x >> 3;
    const int partn = nb >> 2, e0 = (nb & 3) * 144;
    const u16* W = partn ? wim : wre;
    char* sA = smem;
    char* sB = smem + 64 * A1_LDA;
    const u16* src[3];
#pragma unroll
    for (int i = 0; i < 3; ++i) {
        int p = tid + 384 * i, n = p >> 3, c = (p & 7) ^ (n & 7);
        src[i] = W + (size_t)(e0 + n) * D_ + c * 8;
    }
    auto issue = [&](int ks, int buf) {
        char* dst = sB + buf * B1_BB + wid * 1024;
#pragma unroll
        for (int i = 0; i < 3; ++i)
            __builtin_amdgcn_global_load_lds((const unsigned*)(src[i] + ks * 64), (LDS_PTR(unsigned))(dst + i * 6144), 16, 0, 0);
    };
    issue(0, 0); issue(1, 1); issue(2, 2);
    if (tid < 256) ln_rows_to_lds(x, mb * 64, 64, sA, A1_LDA, tid);
    const int wm = wid / 3, wn = wid % 3;
    f32x4 acc[2][3];
#pragma unroll
    for (int m = 0; m < 2; ++m)
#pragma unroll
        for (int n = 0; n < 3; ++n) acc[m][n] = f32x4{0.f, 0.f, 0.f, 0.f};
    for (int ks = 0; ks < 9; ++ks) {
        if (ks < 7) asm volatile("s_waitcnt vmcnt(6)" ::: "memory");
        else if (ks == 7) asm volatile("s_waitcnt vmcnt(3)" ::: "memory");
        else asm volatile("s_waitcnt vmcnt(0)" ::: "memory");
        asm volatile("s_waitcnt lgkmcnt(0)" ::: "memory");
        __builtin_amdgcn_s_barrier();
        if (ks + 3 < 9) issue(ks + 3, (ks + 3) & 3);
        const char* bb = sB + (ks & 3) * B1_BB;
#pragma unroll
        for (int kk = 0; kk < 2; ++kk) {
            bf16x8 af[2];
#pragma unroll
            for (int m = 0; m < 2; ++m)
                af[m] = *reinterpret_cast<const bf16x8*>(sA + (wm * 32 + m * 16 + (lane & 15)) * A1_LDA + ks * 128 + kk * 64 + (lane >> 4) * 16);
#pragma unroll
            for (int n = 0; n < 3; ++n) {
                int nn = (wn * 3 + n) * 16 + (lane & 15), c = kk * 4 + (lane >> 4);
                bf16x8 bf = *reinterpret_cast<const bf16x8*>(bb + nn * 128 + ((c ^ (nn & 7)) * 16));
#pragma unroll
                for (int m = 0; m < 2; ++m) acc[m][n] = mfma16(af[m], bf, acc[m][n]);
            }
        }
    }
#pragma unroll
    for (int m = 0; m < 2; ++m)
#pragma unroll
        for (int n = 0; n < 3; ++n) {
            int e = e0 + (wn * 3 + n) * 16 + (lane & 15);
            int s = mb * 64 + wm * 32 + m * 16 + (lane >> 4) * 4;
            s16x4 v;
#pragma unroll
            for (int r = 0; r < 4; ++r) v[r] = (short)f2bf(acc[m][n][r]);
            *reinterpret_cast<s16x4*>(bt + ((size_t)partn * D_ + e) * S_ + s) = v;
        }
}

constexpr int B2_ST = 16384 + 18432;
constexpr int B2_LDS = 4 * B2_ST;
__global__ __launch_bounds__(256) void k_b2(const u16* __restrict__ cre, const u16* __restrict__ cim,
                                            const u16* __restrict__ bt, float* __restrict__ pp) {
    extern __shared__ __attribute__((aligned(16))) char smem[];
    const int tid = threadIdx.x, lane = tid & 63, wid = tid >> 6;
    const int xcd = blockIdx.x & 7, idx = blockIdx.x >> 3;
    const int kc = xcd >> 1, mt = (xcd & 1) * 8 + (idx >> 2), nt4 = idx & 3;
    const int t0 = mt * 128, n0 = nt4 * 144;
    const u16* Cm = (kc >> 1) ? cim : cre;
    const int kbase = (kc & 1) * 1024;
    const u16* src[9];
#pragma unroll
    for (int i = 0; i < 9; ++i) {
        int P = i * 256 + tid;
        if (i < 4) {
            int row = P >> 3, c = (P & 7) ^ (row & 7);
            src[i] = Cm + (size_t)(t0 + row) * S_ + kbase + c * 8;
        } else {
            int PB = P - 1024; if (PB >= 1152) PB -= 128;
            int row = PB >> 3, c = (PB & 7) ^ (row & 7);
            src[i] = bt + ((size_t)(kc >> 1) * D_ + n0 + row) * S_ + kbase + c * 8;
        }
    }
    const int w8 = (wid >= 2) ? (8 * 4096 + (wid - 2) * 1024) : (8 * 4096 + wid * 1024);
    auto issue = [&](int it, int buf) {
        char* dst = smem + buf * B2_ST;
#pragma unroll
        for (int i = 0; i < 8; ++i)
            __builtin_amdgcn_global_load_lds((const unsigned*)(src[i] + it * 64), (LDS_PTR(unsigned))(dst + i * 4096 + wid * 1024), 16, 0, 0);
        __builtin_amdgcn_global_load_lds((const unsigned*)(src[8] + it * 64), (LDS_PTR(unsigned))(dst + w8), 16, 0, 0);
    };
    f32x4 acc[2][9];
#pragma unroll
    for (int m = 0; m < 2; ++m)
#pragma unroll
        for (int n = 0; n < 9; ++n) acc[m][n] = f32x4{0.f, 0.f, 0.f, 0.f};
    issue(0, 0); issue(1, 1); issue(2, 2);
    for (int it = 0; it < 16; ++it) {
        if (it < 14) asm volatile("s_waitcnt vmcnt(18)" ::: "memory");
        else if (it == 14) asm volatile("s_waitcnt vmcnt(9)" ::: "memory");
        else asm volatile("s_waitcnt vmcnt(0)" ::: "memory");
        asm volatile("s_waitcnt lgkmcnt(0)" ::: "memory");
        __builtin_amdgcn_s_barrier();
        if (it + 3 < 16) issue(it + 3, (it + 3) & 3);
        const char* sa = smem + (it & 3) * B2_ST;
        const char* sbb = sa + 16384;
#pragma unroll
        for (int kk = 0; kk < 2; ++kk) {
            bf16x8 af[2];
#pragma unroll
            for (int m = 0; m < 2; ++m) {
                int row = wid * 32 + m * 16 + (lane & 15), c = kk * 4 + (lane >> 4);
                af[m] = *reinterpret_cast<const bf16x8*>(sa + row * 128 + ((c ^ (row & 7)) * 16));
            }
#pragma unroll
            for (int n = 0; n < 9; ++n) {
                int row = n * 16 + (lane & 15), c = kk * 4 + (lane >> 4);
                bf16x8 bf = *reinterpret_cast<const bf16x8*>(sbb + row * 128 + ((c ^ (row & 7)) * 16));
#pragma unroll
                for (int m = 0; m < 2; ++m) acc[m][n] = mfma16(af[m], bf, acc[m][n]);
            }
        }
    }
    float* dstp = pp + (size_t)kc * S_ * D_;
#pragma unroll
    for (int m = 0; m < 2; ++m)
#pragma unroll
        for (int n = 0; n < 9; ++n)
#pragma unroll
            for (int r = 0; r < 4; ++r) {
                int row = t0 + wid * 32 + m * 16 + (lane >> 4) * 4 + r, col = n0 + n * 16 + (lane & 15);
                dstp[(size_t)row * D_ + col] = acc[m][n][r];
            }
}

__global__ __launch_bounds__(256) void k_red(const float* __restrict__ pp, const u16* __restrict__ x, u16* __restrict__ xo) {
    size_t i = ((size_t)blockIdx.x * 256 + threadIdx.x) * 4;
    const size_t N = (size_t)S_ * D_;
    f32x4 a = *reinterpret_cast<const f32x4*>(pp + i), b = *reinterpret_cast<const f32x4*>(pp + N + i);
    f32x4 c = *reinterpret_cast<const f32x4*>(pp + 2 * N + i), d = *reinterpret_cast<const f32x4*>(pp + 3 * N + i);
    s16x4 xv = *reinterpret_cast<const s16x4*>(x + i), ov;
#pragma unroll
    for (int j = 0; j < 4; ++j) {
        float y = rbf(rbf(a[j] + b[j]) - rbf(c[j] + d[j]));
        ov[j] = (short)f2bf(bf2f((u16)xv[j]) + y);
    }
    *reinterpret_cast<s16x4*>(xo + i) = ov;
}

extern "C" void kernel_launch(void* const* d_in, const int* in_sizes, int n_in, void* d_out, int out_size,
                              void* d_ws, size_t ws_size, hipStream_t stream) {
    const u16* A_x = (const u16*)d_in[0];
    const u16* wqkv = (const u16*)d_in[1];
    const u16* wphi = (const u16*)d_in[2];
    const u16* wo = (const u16*)d_in[3];
    const u16* cre = (const u16*)d_in[5];
    const u16* cim = (const u16*)d_in[6];
    const u16* dre = (const u16*)d_in[7];
    const u16* dim_ = (const u16*)d_in[8];
    char* ws = (char*)d_ws;
    const size_t XB = (size_t)S_ * D_ * 2;
    u16* X1 = (u16*)(ws);
    u16* X2 = (u16*)(ws + XB);
    u16* X3 = (u16*)(ws + 2 * XB);
    u16* O = (u16*)(ws + 3 * XB);
    u16* PHIQ = (u16*)(ws + 4 * XB);
    float* PART = (float*)(ws + 4 * XB + (size_t)S_ * H_ * PHS * 2);
    u16* BT = (u16*)(ws + 4 * XB + (size_t)S_ * H_ * PHS * 2 + (size_t)32 * H_ * 720 * 4);
    u16* XOUT = (u16*)d_out;

    hipFuncSetAttribute((const void*)k_a1, hipFuncAttributeMaxDynamicSharedMemorySize, A1_LDS);
    hipFuncSetAttribute((const void*)k_a2, hipFuncAttributeMaxDynamicSharedMemorySize, A2_LDS);
    hipFuncSetAttribute((const void*)k_b1, hipFuncAttributeMaxDynamicSharedMemorySize, B1_LDS);
    hipFuncSetAttribute((const void*)k_b2, hipFuncAttributeMaxDynamicSharedMemorySize, B2_LDS);
    float* PP = (float*)((char*)BT + (size_t)2 * D_ * S_ * 2);
    const u16* xin = A_x;
    u16* xs[4] = {X1, X2, X3, XOUT};
    for (int layer = 0; layer < 4; ++layer) {
        u16* xo = xs[layer];
        if ((layer & 1) == 0) {
            k_a1<<<256, 256, A1_LDS, stream>>>(xin, wqkv, wphi, PHIQ, PART);
            k_o<<<256, 256, 0, stream>>>(PHIQ, PART, O);
            k_a2<<<256, 384, A2_LDS, stream>>>(O, wo, xin, xo);
        } else {
            k_b1<<<256, 384, B1_LDS, stream>>>(xin, dre, dim_, BT);
            k_b2<<<256, 256, B2_LDS, stream>>>(cre, cim, BT, PP);
            k_red<<<S_ * D_ / 1024, 256, 0, stream>>>(PP, xin, xo);
        }
        xin = xo;
    }
}
```

```cpp
#include <hip/hip_runtime.h>
#include <hip/hip_bf16.h>
#include <stdint.h>

typedef unsigned short u16;
typedef __attribute__((ext_vector_type(8))) short bf16x8;
typedef __attribute__((ext_vector_type(4))) short s16x4;
typedef __attribute__((ext_vector_type(4))) float f32x4;
typedef __attribute__((ext_vector_type(4))) unsigned u32x4;
#define LDS_PTR(T) __attribute__((address_space(3))) T*

#define STAMP(i) do {} while (0)
constexpr int S_ = 2048, D_ = 576, H_ = 32, DH_ = 18;
constexpr int PHS = 40;

__device__ __forceinline__ u16 f2bf(float f) {
    __hip_bfloat16 h = __float2bfloat16(f);
    return *reinterpret_cast<u16*>(&h);
}
__device__ __forceinline__ float bf2f(u16 u) { return __uint_as_float(((unsigned)u) << 16); }
__device__ __forceinline__ float rbf(float f) { return bf2f(f2bf(f)); }
__device__ __forceinline__ f32x4 mfma16(bf16x8 a, bf16x8 b, f32x4 c) {
    return __builtin_amdgcn_mfma_f32_16x16x32_bf16(a, b, c, 0, 0, 0);
}
__device__ __forceinline__ bf16x8 trfrag(const char* base, int rs, int lane) {
    int g = lane >> 4, q = (lane >> 2) & 3, p = lane & 3;
    const char* a0 = base + (8 * g + q) * rs + p * 8;
    s16x4 lo = __builtin_amdgcn_ds_read_tr16_b64_v4i16((LDS_PTR(s16x4))(a0));
    s16x4 hi = __builtin_amdgcn_ds_read_tr16_b64_v4i16((LDS_PTR(s16x4))(a0 + 4 * rs));
    bf16x8 r;
    r[0] = lo[0]; r[1] = lo[1]; r[2] = lo[2]; r[3] = lo[3];
    r[4] = hi[0]; r[5] = hi[1]; r[6] = hi[2]; r[7] = hi[3];
    return r;
}

__device__ __forceinline__ void ln_rows_to_lds(const u16* __restrict__ x, int row0, int nrows, char* lds, int lda, int tid) {
    for (int pass = 0; pass < nrows / 32; ++pass) {
        int r = pass * 32 + (tid >> 3), l8 = tid & 7;
        const u32x4* src = reinterpret_cast<const u32x4*>(x + (size_t)(row0 + r) * D_);
        u32x4 v[9];
#pragma unroll
        for (int i = 0; i < 9; ++i) v[i] = src[l8 + 8 * i];
        float s = 0.f;
#pragma unroll
        for (int i = 0; i < 9; ++i)
#pragma unroll
            for (int j = 0; j < 4; ++j) s += __uint_as_float(v[i][j] << 16) + __uint_as_float(v[i][j] & 0xffff0000u);
        s += __shfl_xor(s, 1); s += __shfl_xor(s, 2); s += __shfl_xor(s, 4);
        float mean = s * (1.0f / D_);
        float q = 0.f;
#pragma unroll
        for (int i = 0; i < 9; ++i)
#pragma unroll
            for (int j = 0; j < 4; ++j) {
                float a = __uint_as_float(v[i][j] << 16) - mean, b = __uint_as_float(v[i][j] & 0xffff0000u) - mean;
                q += a * a + b * b;
            }
        q += __shfl_xor(q, 1); q += __shfl_xor(q, 2); q += __shfl_xor(q, 4);
        float var = q * (1.0f / D_);
        float mb = rbf(mean);
        float sd = rbf(sqrtf(rbf(rbf(var) + 1e-5f)));
        float rs = 1.0f / sd;
#pragma unroll
        for (int i = 0; i < 9; ++i) {
            u32x4 o;
#pragma unroll
            for (int j = 0; j < 4; ++j) {
                float a = rbf(__uint_as_float(v[i][j] << 16) - mb), b = rbf(__uint_as_float(v[i][j] & 0xffff0000u) - mb);
                o[j] = (unsigned)f2bf(a * rs) | ((unsigned)f2bf(b * rs) << 16);
            }
            *reinterpret_cast<u32x4*>(lds + r * lda + (l8 + 8 * i) * 16) = o;
        }
    }
}

__global__ __launch_bounds__(256) void k_prep(const u16* __restrict__ wqkv, u16* __restrict__ wpk) {
    int g = blockIdx.x * 256 + threadIdx.x;
    int p = g & 1023, ks = (g >> 10) % 18, hg = g / (18 * 1024);
    int k = p / 30, c = p % 30;
    if (k > 31) k = 31;
    if (c > 26) c = 26;
    int seg = c / 9, w = c % 9;
    *reinterpret_cast<u32x4*>(wpk + (size_t)g * 8) =
        *reinterpret_cast<const u32x4*>(wqkv + (size_t)(ks * 32 + k) * 1728 + seg * 576 + hg * 72 + w * 8);
}

constexpr int A1_LDA = 1168;
constexpr int A1_RS = 480;
constexpr int A1_BB = 16384;
constexpr int A1_LDS = 64 * A1_LDA + 4 * A1_BB;
__global__ __launch_bounds__(256) void k_a1(const u16* __restrict__ x, const u16* __restrict__ wqkv,
                                            const u16* __restrict__ wphi, u16* __restrict__ phiq,
                                            float* __restrict__ part) {
    extern __shared__ __attribute__((aligned(16))) char smem[];
    const int tid = threadIdx.x, lane = tid & 63, wid = tid >> 6;
    const int hg = blockIdx.x & 7, mb = blockIdx.x >> 3;
    char* sA = smem;
    char* sB = smem + 64 * A1_LDA;
    const u16* wpk = wqkv + (size_t)hg * 18 * 8192 + tid * 8;
    auto issue = [&](int ks, int buf) {
        char* dst = sB + buf * A1_BB + wid * 1024;
#pragma unroll
        for (int i = 0; i < 4; ++i)
            __builtin_amdgcn_global_load_lds((const unsigned*)(wpk + (size_t)ks * 8192 + i * 2048), (LDS_PTR(unsigned))(dst + i * 4096), 16, 0, 0);
    };
    STAMP(0);
    issue(0, 0); issue(1, 1); issue(2, 2);
    ln_rows_to_lds(x, mb * 64, 64, sA, A1_LDA, tid);
    STAMP(1);
    const int wm = wid >> 1, wn = wid & 1;
    f32x4 acc[2][7];
#pragma unroll
    for (int m = 0; m < 2; ++m)
#pragma unroll
        for (int n = 0; n < 7; ++n) acc[m][n] = f32x4{0.f, 0.f, 0.f, 0.f};
    for (int ks = 0; ks < 18; ++ks) {
        if (ks < 16) asm volatile("s_waitcnt vmcnt(8)" ::: "memory");
        else if (ks == 16) asm volatile("s_waitcnt vmcnt(4)" ::: "memory");
        else asm volatile("s_waitcnt vmcnt(0)" ::: "memory");
        asm volatile("s_waitcnt lgkmcnt(0)" ::: "memory");
        __builtin_amdgcn_s_barrier();
        if (ks + 3 < 18) issue(ks + 3, (ks + 3) & 3);
        const char* bb = sB + (ks & 3) * A1_BB;
        bf16x8 af[2], bfv[7];
#pragma unroll
        for (int m = 0; m < 2; ++m)
            af[m] = *reinterpret_cast<const bf16x8*>(sA + (wm * 32 + m * 16 + (lane & 15)) * A1_LDA + ks * 64 + (lane >> 4) * 16);
#pragma unroll
        for (int n = 0; n < 7; ++n) bfv[n] = trfrag(bb + (wn * 7 + n) * 32, A1_RS, lane);
        __builtin_amdgcn_sched_barrier(0);
#pragma unroll
        for (int n = 0; n < 7; ++n)
#pragma unroll
            for (int m = 0; m < 2; ++m) acc[m][n] = mfma16(af[m], bfv[n], acc[m][n]);
    }
    __syncthreads();
    STAMP(2);
    char* Cq = smem;
    float* P = reinterpret_cast<float*>(smem + 49152);
    u16* KT = reinterpret_cast<u16*>(smem + 90112);
    u16* VT = reinterpret_cast<u16*>(smem + 114688);
    {
        u32x4 z = {0u, 0u, 0u, 0u};
        for (int i = tid; i < 49152 / 16; i += 256) *reinterpret_cast<u32x4*>(Cq + i * 16) = z;
        for (int i = tid; i < 40960 / 16; i += 256) *reinterpret_cast<u32x4*>(smem + 90112 + i * 16) = z;
    }
    __syncthreads();
#pragma unroll
    for (int m = 0; m < 2; ++m)
#pragma unroll
        for (int n = 0; n < 7; ++n) {
            int c = (wn * 7 + n) * 16 + (lane & 15);
            if (c < 216) {
                int seg = c / 72, rem = c % 72, h = rem / 18, d = rem % 18;
#pragma unroll
                for (int r = 0; r < 4; ++r) {
                    int row = wm * 32 + m * 16 + (lane >> 4) * 4 + r;
                    *reinterpret_cast<u16*>(Cq + row * 768 + seg * 256 + h * 64 + d * 2) = f2bf(acc[m][n][r]);
                }
            }
        }
    __syncthreads();
    STAMP(3);
    {
        const int h = wid, hglob = hg * 4 + h;
        bf16x8 wb[2];
#pragma unroll
        for (int ft = 0; ft < 2; ++ft)
#pragma unroll
            for (int j = 0; j < 8; ++j) {
                int k = (lane >> 4) * 8 + j, f = ft * 16 + (lane & 15);
                wb[ft][j] = (k < 18 && f < 18) ? (short)wphi[(size_t)hglob * 324 + k * 18 + f] : (short)0;
            }
#pragma unroll
        for (int seg = 0; seg < 2; ++seg)
#pragma unroll
            for (int mt = 0; mt < 4; ++mt) {
                bf16x8 a = *reinterpret_cast<const bf16x8*>(Cq + (mt * 16 + (lane & 15)) * 768 + seg * 256 + h * 64 + (lane >> 4) * 16);
#pragma unroll
                for (int ft = 0; ft < 2; ++ft) {
                    f32x4 c = mfma16(a, wb[ft], f32x4{0.f, 0.f, 0.f, 0.f});
                    int f = ft * 16 + (lane & 15);
                    if (f < 18) {
#pragma unroll
                        for (int r = 0; r < 4; ++r) {
                            int row = mt * 16 + (lane >> 4) * 4 + r;
                            P[((row * 2 + seg) * 4 + h) * 20 + f] = rbf(c[r]);
                        }
                    }
                }
            }
    }
    __syncthreads();
    STAMP(4);
    {
        const int row = tid >> 2, h = tid & 3, hglob = hg * 4 + h;
#pragma unroll
        for (int seg = 0; seg < 2; ++seg) {
            float p[18];
            float mx = -INFINITY, mn = INFINITY;
#pragma unroll
            for (int f = 0; f < 18; ++f) { p[f] = P[((row * 2 + seg) * 4 + h) * 20 + f]; mx = fmaxf(mx, p[f]); mn = fminf(mn, p[f]); }
            float e1[18], e2[18], s1 = 0.f, s2 = 0.f;
#pragma unroll
            for (int f = 0; f < 18; ++f) { e1[f] = __expf(p[f] - mx); e2[f] = __expf(mn - p[f]); s1 += e1[f]; s2 += e2[f]; }
            float r1 = 1.0f / s1, r2 = 1.0f / s2;
            u16 ph[36];
#pragma unroll
            for (int f = 0; f < 18; ++f) { ph[f] = f2bf(e1[f] * r1); ph[18 + f] = f2bf(e2[f] * r2); }
            if (seg == 0) {
                u16* dst = phiq + ((size_t)(mb * 64 + row) * H_ + hglob) * PHS;
#pragma unroll
                for (int f = 0; f < 36; f += 2) *reinterpret_cast<unsigned*>(dst + f) = (unsigned)ph[f] | ((unsigned)ph[f + 1] << 16);
                *reinterpret_cast<unsigned*>(dst + 36) = 0u;
                *reinterpret_cast<unsigned*>(dst + 38) = 0u;
            } else {
#pragma unroll
                for (int f = 0; f < 36; ++f) KT[(h * 48 + f) * 64 + row] = ph[f];
            }
        }
#pragma unroll
        for (int d = 0; d < 18; ++d) VT[(h * 32 + d) * 64 + row] = *reinterpret_cast<const u16*>(Cq + row * 768 + 2 * 256 + h * 64 + d * 2);
        VT[(h * 32 + 18) * 64 + row] = (u16)0x3F80;
    }
    __syncthreads();
    STAMP(5);
    {
        const int h = wid, hglob = hg * 4 + h;
#pragma unroll
        for (int ft = 0; ft < 3; ++ft)
#pragma unroll
            for (int dt = 0; dt < 2; ++dt) {
                f32x4 c = f32x4{0.f, 0.f, 0.f, 0.f};
#pragma unroll
                for (int ks = 0; ks < 2; ++ks) {
                    bf16x8 a = *reinterpret_cast<const bf16x8*>(KT + (h * 48 + ft * 16 + (lane & 15)) * 64 + ks * 32 + (lane >> 4) * 8);
                    bf16x8 b = *reinterpret_cast<const bf16x8*>(VT + (h * 32 + dt * 16 + (lane & 15)) * 64 + ks * 32 + (lane >> 4) * 8);
                    c = mfma16(a, b, c);
                }
                int d = dt * 16 + (lane & 15);
                if (d < 20) {
#pragma unroll
                    for (int r = 0; r < 4; ++r) {
                        int f = ft * 16 + (lane >> 4) * 4 + r;
                        if (f < 36) part[(((size_t)mb * H_ + hglob) * 36 + f) * 20 + d] = c[r];
                    }
                }
            }
    }
    STAMP(6);
}

__global__ __launch_bounds__(256) void k_o(const u16* __restrict__ phiq, const float* __restrict__ part, u16* __restrict__ o) {
    __shared__ __attribute__((aligned(16))) u16 KV[32 * 64];
    const int tid = threadIdx.x, lane = tid & 63, wid = tid >> 6;
    const int h = blockIdx.x >> 3, rc = blockIdx.x & 7;
    STAMP(20);
    for (int i = tid; i < 32 * 64; i += 256) KV[i] = 0;
    __syncthreads();
    for (int i = tid; i < 720; i += 256) {
        int f = i / 20, d = i % 20;
        if (d < 19) {
            float v[32];
#pragma unroll
            for (int m = 0; m < 32; ++m) v[m] = part[((size_t)m * H_ + h) * 720 + i];
            float s = 0.f;
#pragma unroll
            for (int m = 0; m < 32; ++m) s += v[m];
            KV[d * 64 + f] = f2bf(s);
        }
    }
    __syncthreads();
    STAMP(21);
    bf16x8 bfr[2][2];
#pragma unroll
    for (int dt = 0; dt < 2; ++dt)
#pragma unroll
        for (int ks = 0; ks < 2; ++ks)
            bfr[dt][ks] = *reinterpret_cast<const bf16x8*>(KV + (dt * 16 + (lane & 15)) * 64 + ks * 32 + (lane >> 4) * 8);
#pragma unroll
    for (int mt = 0; mt < 4; ++mt) {
        int row0 = rc * 256 + wid * 64 + mt * 16;
        const u16* src = phiq + ((size_t)(row0 + (lane & 15)) * H_ + h) * PHS;
        bf16x8 a0 = *reinterpret_cast<const bf16x8*>(src + (lane >> 4) * 8);
        bf16x8 a1;
        if ((lane >> 4) == 0) a1 = *reinterpret_cast<const bf16x8*>(src + 32);
        else a1 = bf16x8{0, 0, 0, 0, 0, 0, 0, 0};
        f32x4 c0 = f32x4{0.f, 0.f, 0.f, 0.f}, c1 = c0;
        c0 = mfma16(a0, bfr[0][0], c0); c0 = mfma16(a1, bfr[0][1], c0);
        c1 = mfma16(a0, bfr[1][0], c1); c1 = mfma16(a1, bfr[1][1], c1);
#pragma unroll
        for (int r = 0; r < 4; ++r) {
            float den = __shfl(c1[r], (lane & 48) + 2);
            den = rbf(rbf(den) + 1e-6f);
            int row = row0 + (lane >> 4) * 4 + r;
            o[(size_t)row * D_ + h * 18 + (lane & 15)] = f2bf(rbf(c0[r]) / den);
            if ((lane & 15) < 2) o[(size_t)row * D_ + h * 18 + 16 + (lane & 15)] = f2bf(rbf(c1[r]) / den);
        }
    }
    STAMP(22);
}

constexpr int A2_BB = 64 * 288;
constexpr int A2_LDS = 32 * A1_LDA + 4 * A2_BB;
__global__ __launch_bounds__(384) void k_a2(const u16* __restrict__ o, const u16* __restrict__ wo,
                                            const u16* __restrict__ x, u16* __restrict__ xo) {
    extern __shared__ __attribute__((aligned(16))) char smem[];
    const int tid = threadIdx.x, lane = tid & 63, wid = tid >> 6;
    const int nt4 = blockIdx.x & 3, mt = blockIdx.x >> 2;
    char* sA = smem;
    char* sB = smem + 32 * A1_LDA;
    const u16* src[3];
#pragma unroll
    for (int i = 0; i < 3; ++i) {
        int p = tid + 384 * i, k = p / 18, c = p % 18;
        src[i] = wo + (size_t)k * D_ + nt4 * 144 + c * 8;
    }
    auto issue = [&](int ks, int buf) {
        char* dst = sB + buf * A2_BB + wid * 1024;
#pragma unroll
        for (int i = 0; i < 3; ++i)
            __builtin_amdgcn_global_load_lds((const unsigned*)(src[i] + (size_t)ks * 64 * D_), (LDS_PTR(unsigned))(dst + i * 6144), 16, 0, 0);
    };
    issue(0, 0); issue(1, 1); issue(2, 2);
    for (int p = tid; p < 32 * 72; p += 384) {
        int r = p / 72, c = p % 72;
        *reinterpret_cast<u32x4*>(sA + r * A1_LDA + c * 16) = *reinterpret_cast<const u32x4*>(o + (size_t)(mt * 32 + r) * D_ + c * 8);
    }
    const int wm = wid / 3, wn = wid % 3;
    f32x4 acc[3];
#pragma unroll
    for (int n = 0; n < 3; ++n) acc[n] = f32x4{0.f, 0.f, 0.f, 0.f};
    for (int ks = 0; ks < 9; ++ks) {
        if (ks < 7) asm volatile("s_waitcnt vmcnt(6)" ::: "memory");
        else if (ks == 7) asm volatile("s_waitcnt vmcnt(3)" ::: "memory");
        else asm volatile("s_waitcnt vmcnt(0)" ::: "memory");
        asm volatile("s_waitcnt lgkmcnt(0)" ::: "memory");
        __builtin_amdgcn_s_barrier();
        if (ks + 3 < 9) issue(ks + 3, (ks + 3) & 3);
        const char* bb = sB + (ks & 3) * A2_BB;
#pragma unroll
        for (int kk = 0; kk < 2; ++kk) {
            bf16x8 bfv[3];
            bf16x8 af = *reinterpret_cast<const bf16x8*>(sA + (wm * 16 + (lane & 15)) * A1_LDA + ks * 128 + kk * 64 + (lane >> 4) * 16);
#pragma unroll
            for (int n = 0; n < 3; ++n) bfv[n] = trfrag(bb + kk * 32 * 288 + (wn * 3 + n) * 32, 288, lane);
            __builtin_amdgcn_sched_barrier(0);
#pragma unroll
            for (int n = 0; n < 3; ++n) acc[n] = mfma16(af, bfv[n], acc[n]);
        }
    }
#pragma unroll
    for (int n = 0; n < 3; ++n)
#pragma unroll
        for (int r = 0; r < 4; ++r) {
            int row = mt * 32 + wm * 16 + (lane >> 4) * 4 + r, col = nt4 * 144 + (wn * 3 + n) * 16 + (lane & 15);
            size_t idx = (size_t)row * D_ + col;
            xo[idx] = f2bf(bf2f(x[idx]) + rbf(acc[n][r]));
        }
}

constexpr int B1_BB = 144 * 128;
constexpr int B1_LDS = 64 * A1_LDA + 4 * B1_BB;
__global__ __launch_bounds__(384) void k_b1(const u16* __restrict__ x, const u16* __restrict__ wre,
                                            const u16* __restrict__ wim, u16* __restrict__ bt) {
    extern __shared__ __attribute__((aligned(16))) char smem[];
    const int tid = threadIdx.x, lane = tid & 63, wid = tid >> 6;
    const int nb = blockIdx.x & 7, mb = blockIdx.x >> 3;
    const int partn = nb >> 2, e0 = (nb & 3) * 144;
    const u16* W = partn ? wim : wre;
    char* sA = smem;
    char* sB = smem + 64 * A1_LDA;
    const u16* src[3];
#pragma unroll
    for (int i = 0; i < 3; ++i) {
        int p = tid + 384 * i, n = p >> 3, c = (p & 7) ^ (n & 7);
        src[i] = W + (size_t)(e0 + n) * D_ + c * 8;
    }
    auto issue = [&](int ks, int buf) {
        char* dst = sB + buf * B1_BB + wid * 1024;
#pragma unroll
        for (int i = 0; i < 3; ++i)
            __builtin_amdgcn_global_load_lds((const unsigned*)(src[i] + ks * 64), (LDS_PTR(unsigned))(dst + i * 6144), 16, 0, 0);
    };
    issue(0, 0); issue(1, 1); issue(2, 2);
    if (tid < 256) ln_rows_to_lds(x, mb * 64, 64, sA, A1_LDA, tid);
    const int wm = wid / 3, wn = wid % 3;
    f32x4 acc[2][3];
#pragma unroll
    for (int m = 0; m < 2; ++m)
#pragma unroll
        for (int n = 0; n < 3; ++n) acc[m][n] = f32x4{0.f, 0.f, 0.f, 0.f};
    for (int ks = 0; ks < 9; ++ks) {
        if (ks < 7) asm volatile("s_waitcnt vmcnt(6)" ::: "memory");
        else if (ks == 7) asm volatile("s_waitcnt vmcnt(3)" ::: "memory");
        else asm volatile("s_waitcnt vmcnt(0)" ::: "memory");
        asm volatile("s_waitcnt lgkmcnt(0)" ::: "memory");
        __builtin_amdgcn_s_barrier();
        if (ks + 3 < 9) issue(ks + 3, (ks + 3) & 3);
        const char* bb = sB + (ks & 3) * B1_BB;
#pragma unroll
        for (int kk = 0; kk < 2; ++kk) {
            bf16x8 af[2], bfv[3];
#pragma unroll
            for (int m = 0; m < 2; ++m)
                af[m] = *reinterpret_cast<const bf16x8*>(sA + (wm * 32 + m * 16 + (lane & 15)) * A1_LDA + ks * 128 + kk * 64 + (lane >> 4) * 16);
#pragma unroll
            for (int n = 0; n < 3; ++n) {
                int nn = (wn * 3 + n) * 16 + (lane & 15), c = kk * 4 + (lane >> 4);
                bfv[n] = *reinterpret_cast<const bf16x8*>(bb + nn * 128 + ((c ^ (nn & 7)) * 16));
            }
            __builtin_amdgcn_sched_barrier(0);
#pragma unroll
            for (int n = 0; n < 3; ++n)
#pragma unroll
                for (int m = 0; m < 2; ++m) acc[m][n] = mfma16(af[m], bfv[n], acc[m][n]);
        }
    }
#pragma unroll
    for (int m = 0; m < 2; ++m)
#pragma unroll
        for (int n = 0; n < 3; ++n) {
            int e = e0 + (wn * 3 + n) * 16 + (lane & 15);
            int s = mb * 64 + wm * 32 + m * 16 + (lane >> 4) * 4;
            s16x4 v;
#pragma unroll
            for (int r = 0; r < 4; ++r) v[r] = (short)f2bf(acc[m][n][r]);
            *reinterpret_cast<s16x4*>(bt + ((size_t)partn * D_ + e) * S_ + s) = v;
        }
}

constexpr int B2_ST = 16384 + 18432;
constexpr int B2_LDS = 4 * B2_ST;
__global__ __launch_bounds__(256) void k_b2(const u16* __restrict__ cre, const u16* __restrict__ cim,
                                            const u16* __restrict__ bt, float* __restrict__ pp) {
    extern __shared__ __attribute__((aligned(16))) char smem[];
    const int tid = threadIdx.x, lane = tid & 63, wid = tid >> 6;
    const int xcd = blockIdx.x & 7, idx = blockIdx.x >> 3;
    const int kc = xcd >> 1, mt = (xcd & 1) * 8 + (idx >> 2), nt4 = idx & 3;
    const int t0 = mt * 128, n0 = nt4 * 144;
    const u16* Cm = (kc >> 1) ? cim : cre;
    const int kbase = (kc & 1) * 1024;
    const u16* src[9];
#pragma unroll
    for (int i = 0; i < 9; ++i) {
        int P = i * 256 + tid;
        if (i < 4) {
            int row = P >> 3, c = (P & 7) ^ (row & 7);
            src[i] = Cm + (size_t)(t0 + row) * S_ + kbase + c * 8;
        } else {
            int PB = P - 1024; if (PB >= 1152) PB -= 128;
            int row = PB >> 3, c = (PB & 7) ^ (row & 7);
            src[i] = bt + ((size_t)(kc >> 1) * D_ + n0 + row) * S_ + kbase + c * 8;
        }
    }
    const int w8 = (wid >= 2) ? (8 * 4096 + (wid - 2) * 1024) : (8 * 4096 + wid * 1024);
    auto issue = [&](int it, int buf) {
        char* dst = smem + buf * B2_ST;
#pragma unroll
        for (int i = 0; i < 8; ++i)
            __builtin_amdgcn_global_load_lds((const unsigned*)(src[i] + it * 64), (LDS_PTR(unsigned))(dst + i * 4096 + wid * 1024), 16, 0, 0);
        __builtin_amdgcn_global_load_lds((const unsigned*)(src[8] + it * 64), (LDS_PTR(unsigned))(dst + w8), 16, 0, 0);
    };
    f32x4 acc[2][9];
#pragma unroll
    for (int m = 0; m < 2; ++m)
#pragma unroll
        for (int n = 0; n < 9; ++n) acc[m][n] = f32x4{0.f, 0.f, 0.f, 0.f};
    STAMP(10);
    issue(0, 0); issue(1, 1); issue(2, 2);
    for (int it = 0; it < 16; ++it) {
        if (it == 1) STAMP(11);
        if (it < 14) asm volatile("s_waitcnt vmcnt(18)" ::: "memory");
        else if (it == 14) asm volatile("s_waitcnt vmcnt(9)" ::: "memory");
        else asm volatile("s_waitcnt vmcnt(0)" ::: "memory");
        asm volatile("s_waitcnt lgkmcnt(0)" ::: "memory");
        __builtin_amdgcn_s_barrier();
        if (it + 3 < 16) issue(it + 3, (it + 3) & 3);
        const char* sa = smem + (it & 3) * B2_ST;
        const char* sbb = sa + 16384;
#pragma unroll
        for (int kk = 0; kk < 2; ++kk) {
            bf16x8 af[2], bfv[9];
#pragma unroll
            for (int m = 0; m < 2; ++m) {
                int row = wid * 32 + m * 16 + (lane & 15), c = kk * 4 + (lane >> 4);
                af[m] = *reinterpret_cast<const bf16x8*>(sa + row * 128 + ((c ^ (row & 7)) * 16));
            }
#pragma unroll
            for (int n = 0; n < 9; ++n) {
                int row = n * 16 + (lane & 15), c = kk * 4 + (lane >> 4);
                bfv[n] = *reinterpret_cast<const bf16x8*>(sbb + row * 128 + ((c ^ (row & 7)) * 16));
            }
            __builtin_amdgcn_sched_barrier(0);
#pragma unroll
            for (int n = 0; n < 9; ++n)
#pragma unroll
                for (int m = 0; m < 2; ++m) acc[m][n] = mfma16(af[m], bfv[n], acc[m][n]);
        }
    }
    STAMP(12);
    float* dstp = pp + (size_t)kc * S_ * D_;
#pragma unroll
    for (int m = 0; m < 2; ++m)
#pragma unroll
        for (int n = 0; n < 9; ++n)
#pragma unroll
            for (int r = 0; r < 4; ++r) {
                int row = t0 + wid * 32 + m * 16 + (lane >> 4) * 4 + r, col = n0 + n * 16 + (lane & 15);
                dstp[(size_t)row * D_ + col] = acc[m][n][r];
            }
    STAMP(13);
}

__global__ __launch_bounds__(256) void k_red(const float* __restrict__ pp, const u16* __restrict__ x, u16* __restrict__ xo) {
    size_t i = ((size_t)blockIdx.x * 256 + threadIdx.x) * 4;
    const size_t N = (size_t)S_ * D_;
    f32x4 a = *reinterpret_cast<const f32x4*>(pp + i), b = *reinterpret_cast<const f32x4*>(pp + N + i);
    f32x4 c = *reinterpret_cast<const f32x4*>(pp + 2 * N + i), d = *reinterpret_cast<const f32x4*>(pp + 3 * N + i);
    s16x4 xv = *reinterpret_cast<const s16x4*>(x + i), ov;
#pragma unroll
    for (int j = 0; j < 4; ++j) {
        float y = rbf(rbf(a[j] + b[j]) - rbf(c[j] + d[j]));
        ov[j] = (short)f2bf(bf2f((u16)xv[j]) + y);
    }
    *reinterpret_cast<s16x4*>(xo + i) = ov;
}

extern "C" void kernel_launch(void* const* d_in, const int* in_sizes, int n_in, void* d_out, int out_size,
                              void* d_ws, size_t ws_size, hipStream_t stream) {
    const u16* A_x = (const u16*)d_in[0];
    const u16* wqkv = (const u16*)d_in[1];
    const u16* wphi = (const u16*)d_in[2];
    const u16* wo = (const u16*)d_in[3];
    const u16* cre = (const u16*)d_in[5];
    const u16* cim = (const u16*)d_in[6];
    const u16* dre = (const u16*)d_in[7];
    const u16* dim_ = (const u16*)d_in[8];
    char* ws = (char*)d_ws;
    const size_t XB = (size_t)S_ * D_ * 2;
    u16* X1 = (u16*)(ws);
    u16* X2 = (u16*)(ws + XB);
    u16* X3 = (u16*)(ws + 2 * XB);
    u16* O = (u16*)(ws + 3 * XB);
    u16* PHIQ = (u16*)(ws + 4 * XB);
    float* PART = (float*)(ws + 4 * XB + (size_t)S_ * H_ * PHS * 2);
    u16* BT = (u16*)(ws + 4 * XB + (size_t)S_ * H_ * PHS * 2 + (size_t)32 * H_ * 720 * 4);
    u16* XOUT = (u16*)d_out;

    hipFuncSetAttribute((const void*)k_a1, hipFuncAttributeMaxDynamicSharedMemorySize, A1_LDS);
    hipFuncSetAttribute((const void*)k_a2, hipFuncAttributeMaxDynamicSharedMemorySize, A2_LDS);
    hipFuncSetAttribute((const void*)k_b1, hipFuncAttributeMaxDynamicSharedMemorySize, B1_LDS);
    hipFuncSetAttribute((const void*)k_b2, hipFuncAttributeMaxDynamicSharedMemorySize, B2_LDS);
    float* PP = (float*)((char*)BT + (size_t)2 * D_ * S_ * 2);
    u16* WPK = (u16*)((char*)PP + (size_t)4 * S_ * D_ * 4);
    k_prep<<<576, 256, 0, stream>>>(wqkv, WPK);
    const u16* xin = A_x;
    u16* xs[4] = {X1, X2, X3, XOUT};
    for (int layer = 0; layer < 4; ++layer) {
        u16* xo = xs[layer];
        if ((layer & 1) == 0) {
            k_a1<<<256, 256, A1_LDS, stream>>>(xin, WPK, wphi, PHIQ, PART);
            k_o<<<256, 256, 0, stream>>>(PHIQ, PART, O);
            k_a2<<<256, 384, A2_LDS, stream>>>(O, wo, xin, xo);
        } else {
            k_b1<<<256, 384, B1_LDS, stream>>>(xin, dre, dim_, BT);
            k_b2<<<256, 256, B2_LDS, stream>>>(cre, cim, BT, PP);
            k_red<<<S_ * D_ / 1024, 256, 0, stream>>>(PP, xin, xo);
        }
        xin = xo;
    }
}
```
